```python
import torch

def _softmax(x, axis=-1):
    return torch.softmax(x, dim=axis)
def _gelu(x):
    return torch.nn.functional.gelu(x, approximate="tanh")
def _silu(x):
    return torch.nn.functional.silu(x)
def _sigmoid(x):
    return torch.sigmoid(x)
def _layernorm(x, axis=-1, eps=1e-5):
    m = x.mean(dim=axis, keepdim=True)
    v = x.var(dim=axis, keepdim=True, unbiased=False)
    return (x - m) / torch.sqrt(v + eps)
def _scatter_add(tgt, idx, src, axis=0):
    return tgt.clone().index_add_(axis, idx, src)
def _segment_sum(src, seg, n):
    out = torch.zeros((n,) + src.shape[1:], dtype=src.dtype)
    return out.index_add_(0, seg, src)
def _pad(x, pw):
    flat = tuple(v for pair in reversed(pw) for v in pair)
    return torch.nn.functional.pad(x, flat)

kernel_name = 'gated_mix__ssm_conv_h3__softmax_full_mha__prompt'

def setup_inputs():

    _w = (lambda a: (lambda t: t.to(torch.bfloat16) if t.is_floating_point() else t)(torch.as_tensor(a)))
    def _A_setup():
        B, S, D = 1, 2048, 512
        ks = 4
        return {
            "q": _w((lambda *s: torch.randn(*s, dtype=torch.bfloat16) * 0.05)(B, S, D)),
            "k": _w((lambda *s: torch.randn(*s, dtype=torch.bfloat16) * 0.4)(B, S, D)),
            "v": _w((lambda *s: torch.randn(*s, dtype=torch.bfloat16) * 0.4)(B, S, D)),
            "shift_k": _w((lambda *s: torch.randn(*s, dtype=torch.bfloat16) * 0.4)(D, ks) * 0.5),
            "ssm_k": _w((lambda *s: torch.randn(*s, dtype=torch.bfloat16) * 1.6)(D, S) * (1.0 / S ** 0.5)),
            "ssm_D": _w((lambda *s: torch.randn(*s, dtype=torch.bfloat16) * 0.4)(D)),
        }
    _A_in = _A_setup()

    def _B_setup():
        B, S, D = 1, 2048, 512
        return {
            "x": _w((lambda *s: torch.randn(*s, dtype=torch.bfloat16) * 0.05)(B, S, D)),
            "wqkv": _w((lambda *s: torch.randn(*s, dtype=torch.bfloat16) * 0.8)(D, 3 * D)),
            "wo": _w((lambda *s: torch.randn(*s, dtype=torch.bfloat16) * 0.05)(D, D)),
        }
    _B_in = _B_setup()

    return {
        "A_q": _A_in["q"],
        "A_k": _A_in["k"],
        "A_v": _A_in["v"],
        "A_shift_k": _A_in["shift_k"],
        "A_ssm_k": _A_in["ssm_k"],
        "A_ssm_D": _A_in["ssm_D"],
        "B_x": _B_in["x"],
        "B_wqkv": _B_in["wqkv"],
        "B_wo": _B_in["wo"],
        "gate_w": _w((lambda *s: torch.randn(*s, dtype=torch.bfloat16) * 0.05)(512) * 16.0),
    }

def reference(A_q, A_k, A_v, A_shift_k, A_ssm_k, A_ssm_D, B_x, B_wqkv, B_wo, gate_w):
    def _A_ref(q, k, v, shift_k, ssm_k, ssm_D):
        B, S, D = q.shape
        ks = shift_k.shape[-1]
        k_pad = _pad(k, ((0, 0), (ks - 1, 0), (0, 0)))
        k_shift = torch.zeros_like(k)
        for t in range(ks):
            k_shift = k_shift + k_pad[:, t:t + S, :] * shift_k[:, ks - 1 - t][None, None, :]
        kv = k_shift * v
        L = 2 * S
        kv_pad = _pad(kv, ((0, 0), (0, S), (0, 0)))
        kf_pad = _pad(ssm_k, ((0, 0), (0, S)))
        kv_f = (lambda x, axes=(-2,-1): torch.fft.rfft2(x.float(), dim=axes))(kv_pad[:, None, :, :], axes=(1, 2))
        kf_f = (lambda x, axes=(-2,-1): torch.fft.rfft2(x.float(), dim=axes))(kf_pad[:, None, :], axes=(1, 2))
        kf_f = (lambda x, *d: x.permute(*d))(kf_f, 1, 2, 0)
        y_f = kv_f * kf_f[None, :, :, :]
        y = (lambda x, s, axes=(-2,-1): torch.fft.irfft2(x, s=s, dim=axes).to(torch.bfloat16))(y_f, (1, L), axes=(1, 2))[:, 0, :S, :]
        y = y + kv * ssm_D[None, None, :]
        return q * y

    def _B_ref(x, wqkv, wo):
        B, S, D = x.shape
        H = 32; Dh = D // H
        qkv = (lambda x, *d: x.permute(*d))((x @ wqkv).reshape(B, S, 3, H, Dh), 2, 0, 3, 1, 4)
        q, k, v = qkv[0], qkv[1], qkv[2]
        scale = Dh ** -0.5
        scores = torch.einsum("bhqd,bhkd->bhqk", q, k) * scale
        p = _softmax(scores, axis=-1)
        o = torch.einsum("bhqk,bhkd->bhqd", p, v)
        return (lambda x, *d: x.permute(*d))(o, 0, 2, 1, 3).reshape(B, S, D) @ wo

    x = A_q
    g = _sigmoid((x * gate_w).sum(-1, keepdims=True))
    a = _A_ref(x, A_k, A_v, A_shift_k, A_ssm_k, A_ssm_D)
    b = _B_ref(x, B_wqkv, B_wo)
    return g * a + (1 - g) * b
```

```cpp
#include <hip/hip_runtime.h>
#include <hip/hip_bf16.h>
#include <stdint.h>

typedef __attribute__((ext_vector_type(8))) short bf16x8;
typedef __attribute__((ext_vector_type(4))) short s16x4;
typedef __attribute__((ext_vector_type(16))) float f32x16;
typedef __attribute__((ext_vector_type(4))) float f32x4;
typedef __attribute__((ext_vector_type(4))) unsigned u32x4;
typedef __attribute__((ext_vector_type(2))) unsigned u32x2;
typedef __attribute__((ext_vector_type(2))) __bf16 bf16v2;
typedef __attribute__((ext_vector_type(2))) float f32v2;

#define DI __device__ __forceinline__
#define MFMA32(a, b, c) __builtin_amdgcn_mfma_f32_32x32x16_bf16((a), (b), (c), 0, 0, 0)

constexpr int S_LEN = 2048;
constexpr int DM = 512;

DI unsigned pk2(float a, float b) {
  f32v2 v = {a, b};
  bf16v2 r = __builtin_convertvector(v, bf16v2);
  return __builtin_bit_cast(unsigned, r);
}
DI float bf_lo(unsigned u) { return __uint_as_float(u << 16); }
DI float bf_hi(unsigned u) { return __uint_as_float(u & 0xffff0000u); }
DI float bfs(unsigned short u) { return __uint_as_float(((unsigned)u) << 16); }

DI s16x4 tr_read(const void* p) {
  typedef __attribute__((address_space(3))) s16x4* lp;
  return __builtin_amdgcn_ds_read_tr16_b64_v4i16((lp)p);
}

constexpr size_t WS_Q = 0;
constexpr size_t WS_K = WS_Q + 2u * 1024 * 1024;
constexpr size_t WS_V = WS_K + 2u * 1024 * 1024;
constexpr size_t WS_O = WS_V + 2u * 1024 * 1024;
constexpr size_t WS_KV = WS_O + 2u * 1024 * 1024;
constexpr size_t WS_Y = WS_KV + 2u * 1024 * 1024;
constexpr size_t WS_G = WS_Y + 2u * 1024 * 1024;

__global__ __launch_bounds__(256) void k1_kernel(
    const unsigned short* __restrict__ x, const unsigned short* __restrict__ kin,
    const unsigned short* __restrict__ vin, const unsigned short* __restrict__ shiftk,
    const unsigned short* __restrict__ wqkv, const unsigned short* __restrict__ gatew,
    char* __restrict__ ws) {
  __shared__ __attribute__((aligned(16))) char smem[16384 + 12288];
  const int tid = threadIdx.x, lane = tid & 63, w = tid >> 6, r = lane & 31, h = lane >> 5;
  const int bid = blockIdx.x;
  if (bid < 256) {
    const int mt = bid & 15, nt = bid >> 4;
    const int t0 = mt * 128, n0 = nt * 96;
    unsigned short* Qo = (unsigned short*)(ws + WS_Q);
    unsigned short* Ko = (unsigned short*)(ws + WS_K);
    unsigned short* Vo = (unsigned short*)(ws + WS_V);
    char* Xs = smem;
    char* Wsm = smem + 16384;
    u32x4 xr[4], wr[3];
    auto gload = [&](int kt) {
#pragma unroll
      for (int i = 0; i < 4; ++i) {
        int c = tid + 256 * i, row = c >> 3, ch = c & 7;
        xr[i] = *(const u32x4*)(x + (size_t)(t0 + row) * DM + kt * 64 + ch * 8);
      }
#pragma unroll
      for (int i = 0; i < 3; ++i) {
        int c = tid + 256 * i, row = c / 12, ch = c % 12;
        wr[i] = *(const u32x4*)(wqkv + (size_t)(kt * 64 + row) * 1536 + n0 + ch * 8);
      }
    };
    auto lwrite = [&]() {
#pragma unroll
      for (int i = 0; i < 4; ++i) {
        int c = tid + 256 * i, row = c >> 3, ch = c & 7;
        *(u32x4*)(Xs + row * 128 + ((ch ^ (row & 7)) * 16)) = xr[i];
      }
#pragma unroll
      for (int i = 0; i < 3; ++i) {
        int c = tid + 256 * i, row = c / 12, ch = c % 12;
        *(u32x4*)(Wsm + row * 192 + ch * 16) = wr[i];
      }
    };
    f32x16 acc[3];
#pragma unroll
    for (int j = 0; j < 3; ++j)
#pragma unroll
      for (int i = 0; i < 16; ++i) acc[j][i] = 0.f;
    gload(0);
    const int xrow = 32 * w + r;
    const int i16 = lane & 15, q4 = i16 >> 2, p4 = i16 & 3, blk = (lane >> 4) & 1;
    const bool isv0 = (3 * nt + 0) >= 32, isv1 = (3 * nt + 1) >= 32, isv2 = (3 * nt + 2) >= 32;
    for (int kt = 0; kt < 8; ++kt) {
      __syncthreads();
      lwrite();
      __syncthreads();
      if (kt + 1 < 8) gload(kt + 1);
#pragma unroll
      for (int ks = 0; ks < 4; ++ks) {
        bf16x8 xf = *(const bf16x8*)(Xs + xrow * 128 + (((2 * ks + h) ^ (xrow & 7)) * 16));
#pragma unroll
        for (int j = 0; j < 3; ++j) {
          const char* wp = Wsm + (16 * ks + 8 * h + q4) * 192 + (32 * j + 16 * blk + 4 * p4) * 2;
          s16x4 lo = tr_read(wp);
          s16x4 hi = tr_read(wp + 4 * 192);
          bf16x8 wf = __builtin_shufflevector(lo, hi, 0, 1, 2, 3, 4, 5, 6, 7);
          bool isv = j == 0 ? isv0 : (j == 1 ? isv1 : isv2);
          if (isv) acc[j] = MFMA32(xf, wf, acc[j]);
          else acc[j] = MFMA32(wf, xf, acc[j]);
        }
      }
    }
    const int tile = (t0 >> 5) + w;
#pragma unroll
    for (int j = 0; j < 3; ++j) {
      const int sub = 3 * nt + j;
      const int which = sub >> 4;
      const int hb = (sub & 15) * 2;
      if (which == 0) {
        const float sc = 0.25f * 1.4426950408889634f;
#pragma unroll
        for (int g = 0; g < 4; ++g) {
          int head = hb + (g >> 1), d = 8 * (g & 1) + 4 * h;
          u32x2 v = {pk2(acc[j][4 * g] * sc, acc[j][4 * g + 1] * sc),
                     pk2(acc[j][4 * g + 2] * sc, acc[j][4 * g + 3] * sc)};
          *(u32x2*)(Qo + ((size_t)head * S_LEN + t0 + 32 * w + r) * 16 + d) = v;
        }
      } else if (which == 1) {
#pragma unroll
        for (int g = 0; g < 4; ++g) {
          int head = hb + (g >> 1), hh = g & 1;
          u32x2 v = {pk2(acc[j][4 * g], acc[j][4 * g + 1]), pk2(acc[j][4 * g + 2], acc[j][4 * g + 3])};
          *(u32x2*)(Ko + (((size_t)head * 64 + tile) * 2 + hh) * 256 + r * 8 + 4 * h) = v;
        }
      } else {
        int head = hb + (r >> 4), d = r & 15;
#pragma unroll
        for (int s = 0; s < 2; ++s) {
          u32x4 v = {pk2(acc[j][8 * s], acc[j][8 * s + 1]), pk2(acc[j][8 * s + 2], acc[j][8 * s + 3]),
                     pk2(acc[j][8 * s + 4], acc[j][8 * s + 5]), pk2(acc[j][8 * s + 6], acc[j][8 * s + 7])};
          *(u32x4*)(Vo + ((((size_t)head * 64 + tile) * 2 + s) * 2 + h) * 128 + d * 8) = v;
        }
      }
    }
  } else if (bid < 512) {
    const int pb = bid - 256, tt = pb & 31, ct = pb >> 5;
    unsigned short* Ts = (unsigned short*)smem;
    unsigned short* kvT = (unsigned short*)(ws + WS_KV);
    const int c8 = tid & 7, tl0 = tid >> 3;
    const int cbase = ct * 64 + c8 * 8;
    float sk[8][4];
#pragma unroll
    for (int i = 0; i < 8; ++i) {
      u32x2 sv = *(const u32x2*)(shiftk + (size_t)(cbase + i) * 4);
      sk[i][0] = bf_lo(sv[0]); sk[i][1] = bf_hi(sv[0]); sk[i][2] = bf_lo(sv[1]); sk[i][3] = bf_hi(sv[1]);
    }
#pragma unroll
    for (int half = 0; half < 2; ++half) {
      int tl = tl0 + 32 * half, t = tt * 64 + tl;
      float acc[8];
#pragma unroll
      for (int i = 0; i < 8; ++i) acc[i] = 0.f;
#pragma unroll
      for (int j = 0; j < 4; ++j) {
        if (t - j >= 0) {
          u32x4 kk = *(const u32x4*)(kin + (size_t)(t - j) * DM + cbase);
#pragma unroll
          for (int i = 0; i < 4; ++i) {
            acc[2 * i] += bf_lo(kk[i]) * sk[2 * i][j];
            acc[2 * i + 1] += bf_hi(kk[i]) * sk[2 * i + 1][j];
          }
        }
      }
      u32x4 vv = *(const u32x4*)(vin + (size_t)t * DM + cbase);
#pragma unroll
      for (int i = 0; i < 4; ++i) {
        unsigned p = pk2(acc[2 * i] * bf_lo(vv[i]), acc[2 * i + 1] * bf_hi(vv[i]));
        Ts[(c8 * 8 + 2 * i) * 72 + tl] = (unsigned short)(p & 0xffff);
        Ts[(c8 * 8 + 2 * i + 1) * 72 + tl] = (unsigned short)(p >> 16);
      }
    }
    __syncthreads();
    {
      int cl = tid >> 2, part = tid & 3;
      const u32x4* src = (const u32x4*)(Ts + cl * 72 + part * 16);
      u32x4 a = src[0], b = src[1];
      u32x4* dst = (u32x4*)(kvT + (size_t)(ct * 64 + cl) * S_LEN + tt * 64 + part * 16);
      dst[0] = a; dst[1] = b;
    }
  } else {
    const int gb = bid - 512;
    float* G = (float*)(ws + WS_G);
    u32x4 gw = *(const u32x4*)(gatew + lane * 8);
    for (int i = 0; i < 16; ++i) {
      int t = gb * 64 + w * 16 + i;
      u32x4 xv = *(const u32x4*)(x + (size_t)t * DM + lane * 8);
      float s = 0.f;
#pragma unroll
      for (int k = 0; k < 4; ++k) s += bf_lo(xv[k]) * bf_lo(gw[k]) + bf_hi(xv[k]) * bf_hi(gw[k]);
#pragma unroll
      for (int off = 32; off >= 1; off >>= 1) s += __shfl_xor(s, off);
      if (lane == 0) G[t] = 1.f / (1.f + __expf(-s));
    }
  }
}

constexpr int K2_LDS = 78848;
constexpr float ATT_THR = 8.0f;

__global__ __launch_bounds__(512) void k2_kernel(const unsigned short* __restrict__ ssmk, char* __restrict__ ws) {
  __shared__ __attribute__((aligned(16))) char smem[K2_LDS];
  const int tid = threadIdx.x, lane = tid & 63, w = tid >> 6, r = lane & 31, h = lane >> 5;
  const int bid = blockIdx.x;
  if (bid < 256) {
    const int head = (bid & 7) * 4 + ((bid >> 3) & 3), qb = bid >> 5;
    const unsigned short* Qg = (const unsigned short*)(ws + WS_Q);
    const unsigned short* Kg = (const unsigned short*)(ws + WS_K) + (size_t)head * 64 * 512;
    const unsigned short* Vg = (const unsigned short*)(ws + WS_V) + (size_t)head * 64 * 512;
    unsigned short* Og = (unsigned short*)(ws + WS_O);
    const int q0 = qb * 256 + w * 32;
    bf16x8 qf = *(const bf16x8*)(Qg + ((size_t)head * S_LEN + q0 + r) * 16 + 8 * h);
    for (int i = tid; i < 2 * 8 * 4 * 16; i += 512) {
      int b = i >> 9, rem = i & 511, tsh = rem >> 4, row = 16 + (rem & 15);
      unsigned one = (row == 16) ? 0x3F803F80u : 0u;
      u32x4 v = {one, one, one, one};
      *(u32x4*)(smem + b * 24576 + 8192 + (tsh * 32 + row) * 16) = v;
    }
    u32x4 kr, vr;
    auto gload = [&](int ck) {
      kr = *(const u32x4*)(Kg + (size_t)ck * 4096 + tid * 8);
      vr = *(const u32x4*)(Vg + (size_t)ck * 4096 + tid * 8);
    };
    auto lwrite = [&](int b) {
      *(u32x4*)(smem + b * 24576 + tid * 16) = kr;
      int tsh = tid >> 4, d = tid & 15;
      *(u32x4*)(smem + b * 24576 + 8192 + (tsh * 32 + d) * 16) = vr;
    };
    gload(0);
    lwrite(0);
    gload(1);
    __syncthreads();
    f32x16 o, cm;
#pragma unroll
    for (int i = 0; i < 16; ++i) { o[i] = 0.f; }
    float m;
    {
      bf16x8 kf = *(const bf16x8*)(smem + (h * 32 + r) * 16);
      f32x16 z;
#pragma unroll
      for (int i = 0; i < 16; ++i) z[i] = 0.f;
      f32x16 s = MFMA32(kf, qf, z);
      float t = s[0];
#pragma unroll
      for (int i = 1; i < 16; ++i) t = fmaxf(t, s[i]);
      t = fmaxf(t, __shfl_xor(t, 32));
      m = t;
#pragma unroll
      for (int i = 0; i < 16; ++i) cm[i] = -m;
    }
    for (int ck = 0; ck < 8; ++ck) {
      const char* kb = smem + (ck & 1) * 24576;
#pragma unroll 2
      for (int t = 0; t < 8; ++t) {
        bf16x8 kf = *(const bf16x8*)(kb + t * 1024 + (h * 32 + r) * 16);
        bf16x8 vf0 = *(const bf16x8*)(kb + 8192 + ((t * 2 + 0) * 2 + h) * 512 + r * 16);
        bf16x8 vf1 = *(const bf16x8*)(kb + 8192 + ((t * 2 + 1) * 2 + h) * 512 + r * 16);
        f32x16 s = MFMA32(kf, qf, cm);
        float tm = fmaxf(fmaxf(s[0], s[1]), s[2]);
#pragma unroll
        for (int i = 3; i < 15; i += 2) tm = fmaxf(fmaxf(tm, s[i]), s[i + 1]);
        tm = fmaxf(tm, s[15]);
        if (__any(tm > ATT_THR)) {
          float tp = fmaxf(tm, __shfl_xor(tm, 32));
          float dl = fmaxf(tp, 0.f);
          float al = __builtin_amdgcn_exp2f(-dl);
          m += dl;
#pragma unroll
          for (int i = 0; i < 16; ++i) { o[i] *= al; s[i] -= dl; cm[i] = -m; }
        }
        u32x4 p0, p1;
#pragma unroll
        for (int i = 0; i < 4; ++i) {
          p0[i] = pk2(__builtin_amdgcn_exp2f(s[2 * i]), __builtin_amdgcn_exp2f(s[2 * i + 1]));
          p1[i] = pk2(__builtin_amdgcn_exp2f(s[8 + 2 * i]), __builtin_amdgcn_exp2f(s[8 + 2 * i + 1]));
        }
        o = MFMA32(vf0, __builtin_bit_cast(bf16x8, p0), o);
        o = MFMA32(vf1, __builtin_bit_cast(bf16x8, p1), o);
      }
      if (ck + 1 < 8) {
        __syncthreads();
        lwrite((ck + 1) & 1);
        if (ck + 2 < 8) gload(ck + 2);
        __syncthreads();
      }
    }
    float l = __shfl(o[8], r);
    float inv = 1.f / l;
#pragma unroll
    for (int g = 0; g < 2; ++g) {
      u32x2 v = {pk2(o[4 * g] * inv, o[4 * g + 1] * inv), pk2(o[4 * g + 2] * inv, o[4 * g + 3] * inv)};
      *(u32x2*)(Og + (size_t)(q0 + r) * DM + head * 16 + 8 * g + 4 * h) = v;
    }
  } else {
    const int cb = bid - 256, cg = w >> 2, wi = w & 3, tg = tid & 255;
    const int c = 2 * cb + cg;
    char* base = smem + cg * 39424;
    unsigned short* Ac = (unsigned short*)base;
    char* Us = base + 33280;
    const unsigned short* hg = ssmk + (size_t)c * S_LEN;
    const unsigned short* ug = (const unsigned short*)(ws + WS_KV) + (size_t)c * S_LEN;
    for (int mm = tg; mm < 260; mm += 256) {
      int i0 = 2032 - 8 * mm, i1 = 2040 - 8 * mm;
      u32x4 a = {0, 0, 0, 0}, b = {0, 0, 0, 0};
      if (i0 >= 0) a = *(const u32x4*)(hg + i0);
      if (i1 >= 0) b = *(const u32x4*)(hg + i1);
      unsigned e[9];
#pragma unroll
      for (int i = 0; i < 4; ++i) {
        e[i] = __builtin_amdgcn_alignbit(b[3 - i], b[3 - i], 16);
        e[4 + i] = __builtin_amdgcn_alignbit(a[3 - i], a[3 - i], 16);
      }
      e[8] = 0;
#pragma unroll
      for (int k = 0; k < 8; ++k) {
        u32x4 v;
        if (k & 1) {
#pragma unroll
          for (int i = 0; i < 4; ++i) v[i] = __builtin_amdgcn_alignbit(e[(k >> 1) + i + 1], e[(k >> 1) + i], 16);
        } else {
#pragma unroll
          for (int i = 0; i < 4; ++i) v[i] = e[(k >> 1) + i];
        }
        *(u32x4*)(Ac + k * 2080 + 8 * mm) = v;
      }
    }
    {
      u32x4 uv = *(const u32x4*)(ug + tg * 8);
      int row = (tg >> 2) + 32, ch = tg & 3;
      *(u32x4*)(Us + row * 64 + ((ch ^ ((row >> 2) & 3)) * 16)) = uv;
      if (tg < 128) { u32x4 z = {0, 0, 0, 0}; *(u32x4*)(Us + tg * 16) = z; }
    }
    __syncthreads();
    f32x16 a0, a1;
#pragma unroll
    for (int i = 0; i < 16; ++i) { a0[i] = 0.f; a1[i] = 0.f; }
    const unsigned short* ap = Ac + (7 - (r & 7)) * 2080 + 2040 - 8 * (r >> 3) + 8 * h;
    for (int e = wi; e < 64; e += 4) {
#pragma unroll
      for (int s = 0; s < 2; ++s) {
        bf16x8 af = *(const bf16x8*)(ap - 32 * e + 16 * s);
        int ch = 2 * s + h;
        int row1 = 64 + r - e;
        bf16x8 b1 = *(const bf16x8*)(Us + row1 * 64 + ((ch ^ ((row1 >> 2) & 3)) * 16));
        if (e < 32) {
          int row0 = 32 + r - e;
          bf16x8 b0 = *(const bf16x8*)(Us + row0 * 64 + ((ch ^ ((row0 >> 2) & 3)) * 16));
          a0 = MFMA32(af, b0, a0);
        }
        a1 = MFMA32(af, b1, a1);
      }
    }
    __syncthreads();
    float* red = (float*)base;
#pragma unroll
    for (int g = 0; g < 4; ++g) {
      f32x4 v0 = {a0[4 * g], a0[4 * g + 1], a0[4 * g + 2], a0[4 * g + 3]};
      f32x4 v1 = {a1[4 * g], a1[4 * g + 1], a1[4 * g + 2], a1[4 * g + 3]};
      *(f32x4*)(red + wi * 2048 + r * 32 + 8 * g + 4 * h) = v0;
      *(f32x4*)(red + wi * 2048 + (32 + r) * 32 + 8 * g + 4 * h) = v1;
    }
    __syncthreads();
    {
      f32x4 s0 = {0, 0, 0, 0}, s1 = {0, 0, 0, 0};
#pragma unroll
      for (int k = 0; k < 4; ++k) {
        s0 += *(const f32x4*)(red + k * 2048 + tg * 8);
        s1 += *(const f32x4*)(red + k * 2048 + tg * 8 + 4);
      }
      u32x4 v = {pk2(s0[0], s0[1]), pk2(s0[2], s0[3]), pk2(s1[0], s1[1]), pk2(s1[2], s1[3])};
      *(u32x4*)((unsigned short*)(ws + WS_Y) + (size_t)c * S_LEN + tg * 8) = v;
    }
  }
}

__global__ __launch_bounds__(256) void k3_kernel(const unsigned short* __restrict__ x,
                                                 const unsigned short* __restrict__ wo,
                                                 const unsigned short* __restrict__ ssmD,
                                                 const char* __restrict__ ws, unsigned short* __restrict__ out) {
  __shared__ __attribute__((aligned(16))) char smem[8192 + 8192];
  const int tid = threadIdx.x, lane = tid & 63, w = tid >> 6, r = lane & 31, h = lane >> 5;
  const int bid = blockIdx.x;
  const int mt = bid & 31, nt = bid >> 5;
  const int t0 = mt * 64, n0 = nt * 64;
  const unsigned short* Og = (const unsigned short*)(ws + WS_O);
  char* Xs = smem;
  char* Wsm = smem + 8192;
  u32x4 xr[2], wr[2];
  auto gload = [&](int kt) {
#pragma unroll
    for (int i = 0; i < 2; ++i) {
      int c = tid + 256 * i, row = c >> 3, ch = c & 7;
      xr[i] = *(const u32x4*)(Og + (size_t)(t0 + row) * DM + kt * 64 + ch * 8);
      wr[i] = *(const u32x4*)(wo + (size_t)(kt * 64 + row) * DM + n0 + ch * 8);
    }
  };
  auto lwrite = [&]() {
#pragma unroll
    for (int i = 0; i < 2; ++i) {
      int c = tid + 256 * i, row = c >> 3, ch = c & 7;
      *(u32x4*)(Xs + row * 128 + ((ch ^ (row & 7)) * 16)) = xr[i];
      *(u32x4*)(Wsm + row * 128 + ch * 16) = wr[i];
    }
  };
  f32x16 acc;
#pragma unroll
  for (int i = 0; i < 16; ++i) acc[i] = 0.f;
  gload(0);
  const int wm = w & 1, wn = w >> 1;
  const int xrow = 32 * wm + r;
  const int i16 = lane & 15, q4 = i16 >> 2, p4 = i16 & 3, blk = (lane >> 4) & 1;
  for (int kt = 0; kt < 8; ++kt) {
    __syncthreads();
    lwrite();
    __syncthreads();
    if (kt + 1 < 8) gload(kt + 1);
#pragma unroll
    for (int ks = 0; ks < 4; ++ks) {
      bf16x8 xf = *(const bf16x8*)(Xs + xrow * 128 + (((2 * ks + h) ^ (xrow & 7)) * 16));
      const char* wp = Wsm + (16 * ks + 8 * h + q4) * 128 + (32 * wn + 16 * blk + 4 * p4) * 2;
      s16x4 lo = tr_read(wp);
      s16x4 hi = tr_read(wp + 4 * 128);
      bf16x8 wf = __builtin_shufflevector(lo, hi, 0, 1, 2, 3, 4, 5, 6, 7);
      acc = MFMA32(wf, xf, acc);
    }
  }
  const int t = t0 + 32 * wm + r;
  const float g = ((const float*)(ws + WS_G))[t];
  const unsigned short* yT = (const unsigned short*)(ws + WS_Y);
  const unsigned short* kvT = (const unsigned short*)(ws + WS_KV);
#pragma unroll
  for (int gq = 0; gq < 4; ++gq) {
    int c = n0 + 32 * wn + 8 * gq + 4 * h;
    u32x2 xv = *(const u32x2*)(x + (size_t)t * DM + c);
    u32x2 dv = *(const u32x2*)(ssmD + c);
    float res[4];
#pragma unroll
    for (int i = 0; i < 4; ++i) {
      float xx = (i & 1) ? bf_hi(xv[i >> 1]) : bf_lo(xv[i >> 1]);
      float dd = (i & 1) ? bf_hi(dv[i >> 1]) : bf_lo(dv[i >> 1]);
      float y = bfs(yT[(size_t)(c + i) * S_LEN + t]);
      float kv = bfs(kvT[(size_t)(c + i) * S_LEN + t]);
      float a = xx * (y + kv * dd);
      res[i] = g * a + (1.f - g) * acc[4 * gq + i];
    }
    u32x2 v = {pk2(res[0], res[1]), pk2(res[2], res[3])};
    *(u32x2*)(out + (size_t)t * DM + c) = v;
  }
}

extern "C" void kernel_launch(void* const* d_in, const int* in_sizes, int n_in, void* d_out, int out_size,
                              void* d_ws, size_t ws_size, hipStream_t stream) {
  const unsigned short* x = (const unsigned short*)d_in[0];
  const unsigned short* kin = (const unsigned short*)d_in[1];
  const unsigned short* vin = (const unsigned short*)d_in[2];
  const unsigned short* shiftk = (const unsigned short*)d_in[3];
  const unsigned short* ssmk = (const unsigned short*)d_in[4];
  const unsigned short* ssmD = (const unsigned short*)d_in[5];
  const unsigned short* wqkv = (const unsigned short*)d_in[7];
  const unsigned short* wo = (const unsigned short*)d_in[8];
  const unsigned short* gatew = (const unsigned short*)d_in[9];
  char* ws = (char*)d_ws;
  k1_kernel<<<544, 256, 0, stream>>>(x, kin, vin, shiftk, wqkv, gatew, ws);
  k2_kernel<<<512, 512, 0, stream>>>(ssmk, ws);
  k3_kernel<<<256, 256, 0, stream>>>(x, wo, ssmD, ws, (unsigned short*)d_out);
}
```

```cpp
#include <hip/hip_runtime.h>
#include <hip/hip_bf16.h>
#include <stdint.h>

typedef __attribute__((ext_vector_type(8))) short bf16x8;
typedef __attribute__((ext_vector_type(4))) short s16x4;
typedef __attribute__((ext_vector_type(16))) float f32x16;
typedef __attribute__((ext_vector_type(4))) float f32x4;
typedef __attribute__((ext_vector_type(4))) unsigned u32x4;
typedef __attribute__((ext_vector_type(2))) unsigned u32x2;
typedef __attribute__((ext_vector_type(2))) __bf16 bf16v2;
typedef __attribute__((ext_vector_type(2))) float f32v2;

#define DI __device__ __forceinline__
#define MFMA32(a, b, c) __builtin_amdgcn_mfma_f32_32x32x16_bf16((a), (b), (c), 0, 0, 0)

constexpr int S_LEN = 2048;
constexpr int DM = 512;

DI unsigned pk2(float a, float b) {
  f32v2 v = {a, b};
  bf16v2 r = __builtin_convertvector(v, bf16v2);
  return __builtin_bit_cast(unsigned, r);
}
DI float bf_lo(unsigned u) { return __uint_as_float(u << 16); }
DI float bf_hi(unsigned u) { return __uint_as_float(u & 0xffff0000u); }
DI float bfs(unsigned short u) { return __uint_as_float(((unsigned)u) << 16); }

DI s16x4 tr_read(const void* p) {
  typedef __attribute__((address_space(3))) s16x4* lp;
  return __builtin_amdgcn_ds_read_tr16_b64_v4i16((lp)p);
}

constexpr size_t WS_Q = 0;
constexpr size_t WS_K = WS_Q + 2u * 1024 * 1024;
constexpr size_t WS_V = WS_K + 2u * 1024 * 1024;
constexpr size_t WS_O = WS_V + 2u * 1024 * 1024;
constexpr size_t WS_KV = WS_O + 2u * 1024 * 1024;
constexpr size_t WS_Y = WS_KV + 2u * 1024 * 1024;
constexpr size_t WS_G = WS_Y + 2u * 1024 * 1024;

__global__ __launch_bounds__(256) void k1_kernel(
    const unsigned short* __restrict__ x, const unsigned short* __restrict__ kin,
    const unsigned short* __restrict__ vin, const unsigned short* __restrict__ shiftk,
    const unsigned short* __restrict__ wqkv, const unsigned short* __restrict__ gatew,
    char* __restrict__ ws) {
  __shared__ __attribute__((aligned(16))) char smem[32768 + 24576];
  const int tid = threadIdx.x, lane = tid & 63, w = tid >> 6, r = lane & 31, h = lane >> 5;
  const int bid = blockIdx.x;
  if (bid < 256) {
    const int mt = bid & 15, nt = bid >> 4;
    const int t0 = mt * 128, n0 = nt * 96;
    unsigned short* Qo = (unsigned short*)(ws + WS_Q);
    unsigned short* Ko = (unsigned short*)(ws + WS_K);
    unsigned short* Vo = (unsigned short*)(ws + WS_V);
    char* Xs = smem;
    char* Wsm = smem + 32768;
    u32x4 xr[2][8], wr[2][6];
    auto gload = [&](int kt, u32x4* xa, u32x4* wa) {
#pragma unroll
      for (int i = 0; i < 8; ++i) {
        int c = tid + 256 * i, row = c >> 4, ch = c & 15;
        xa[i] = *(const u32x4*)(x + (size_t)(t0 + row) * DM + kt * 128 + ch * 8);
      }
#pragma unroll
      for (int i = 0; i < 6; ++i) {
        int c = tid + 256 * i, row = c / 12, ch = c % 12;
        wa[i] = *(const u32x4*)(wqkv + (size_t)(kt * 128 + row) * 1536 + n0 + ch * 8);
      }
    };
    auto lwrite = [&](const u32x4* xa, const u32x4* wa) {
#pragma unroll
      for (int i = 0; i < 8; ++i) {
        int c = tid + 256 * i, row = c >> 4, ch = c & 15;
        *(u32x4*)(Xs + row * 256 + ((ch ^ (row & 15)) * 16)) = xa[i];
      }
#pragma unroll
      for (int i = 0; i < 6; ++i) {
        int c = tid + 256 * i, row = c / 12, ch = c % 12;
        *(u32x4*)(Wsm + row * 192 + ch * 16) = wa[i];
      }
    };
    f32x16 acc[3];
#pragma unroll
    for (int j = 0; j < 3; ++j)
#pragma unroll
      for (int i = 0; i < 16; ++i) acc[j][i] = 0.f;
    gload(0, xr[0], wr[0]);
    gload(1, xr[1], wr[1]);
    const int xrow = 32 * w + r;
    const int i16 = lane & 15, q4 = i16 >> 2, p4 = i16 & 3, blk = (lane >> 4) & 1;
    const bool isv0 = (3 * nt + 0) >= 32, isv1 = (3 * nt + 1) >= 32, isv2 = (3 * nt + 2) >= 32;
#pragma unroll
    for (int kt = 0; kt < 4; ++kt) {
      __syncthreads();
      lwrite(xr[kt & 1], wr[kt & 1]);
      __syncthreads();
      if (kt + 2 < 4) gload(kt + 2, xr[kt & 1], wr[kt & 1]);
#pragma unroll
      for (int ks = 0; ks < 8; ++ks) {
        bf16x8 xf = *(const bf16x8*)(Xs + xrow * 256 + (((2 * ks + h) ^ (xrow & 15)) * 16));
#pragma unroll
        for (int j = 0; j < 3; ++j) {
          const char* wp = Wsm + (16 * ks + 8 * h + q4) * 192 + (32 * j + 16 * blk + 4 * p4) * 2;
          s16x4 lo = tr_read(wp);
          s16x4 hi = tr_read(wp + 4 * 192);
          bf16x8 wf = __builtin_shufflevector(lo, hi, 0, 1, 2, 3, 4, 5, 6, 7);
          bool isv = j == 0 ? isv0 : (j == 1 ? isv1 : isv2);
          if (isv) acc[j] = MFMA32(xf, wf, acc[j]);
          else acc[j] = MFMA32(wf, xf, acc[j]);
        }
      }
    }
    const int tile = (t0 >> 5) + w;
#pragma unroll
    for (int j = 0; j < 3; ++j) {
      const int sub = 3 * nt + j;
      const int which = sub >> 4;
      const int hb = (sub & 15) * 2;
      if (which == 0) {
        const float sc = 0.25f * 1.4426950408889634f;
#pragma unroll
        for (int g = 0; g < 4; ++g) {
          int head = hb + (g >> 1), d = 8 * (g & 1) + 4 * h;
          u32x2 v = {pk2(acc[j][4 * g] * sc, acc[j][4 * g + 1] * sc),
                     pk2(acc[j][4 * g + 2] * sc, acc[j][4 * g + 3] * sc)};
          *(u32x2*)(Qo + ((size_t)head * S_LEN + t0 + 32 * w + r) * 16 + d) = v;
        }
      } else if (which == 1) {
#pragma unroll
        for (int g = 0; g < 4; ++g) {
          int head = hb + (g >> 1), hh = g & 1;
          u32x2 v = {pk2(acc[j][4 * g], acc[j][4 * g + 1]), pk2(acc[j][4 * g + 2], acc[j][4 * g + 3])};
          *(u32x2*)(Ko + (((size_t)head * 64 + tile) * 2 + hh) * 256 + r * 8 + 4 * h) = v;
        }
      } else {
        int head = hb + (r >> 4), d = r & 15;
#pragma unroll
        for (int s = 0; s < 2; ++s) {
          u32x4 v = {pk2(acc[j][8 * s], acc[j][8 * s + 1]), pk2(acc[j][8 * s + 2], acc[j][8 * s + 3]),
                     pk2(acc[j][8 * s + 4], acc[j][8 * s + 5]), pk2(acc[j][8 * s + 6], acc[j][8 * s + 7])};
          *(u32x4*)(Vo + ((((size_t)head * 64 + tile) * 2 + s) * 2 + h) * 128 + d * 8) = v;
        }
      }
    }
  } else if (bid < 512) {
    const int pb = bid - 256, tt = pb & 31, ct = pb >> 5;
    unsigned short* Ts = (unsigned short*)smem;
    unsigned short* kvT = (unsigned short*)(ws + WS_KV);
    const int c8 = tid & 7, tl0 = tid >> 3;
    const int cbase = ct * 64 + c8 * 8;
    float sk[8][4];
#pragma unroll
    for (int i = 0; i < 8; ++i) {
      u32x2 sv = *(const u32x2*)(shiftk + (size_t)(cbase + i) * 4);
      sk[i][0] = bf_lo(sv[0]); sk[i][1] = bf_hi(sv[0]); sk[i][2] = bf_lo(sv[1]); sk[i][3] = bf_hi(sv[1]);
    }
#pragma unroll
    for (int half = 0; half < 2; ++half) {
      int tl = tl0 + 32 * half, t = tt * 64 + tl;
      float acc[8];
#pragma unroll
      for (int i = 0; i < 8; ++i) acc[i] = 0.f;
#pragma unroll
      for (int j = 0; j < 4; ++j) {
        if (t - j >= 0) {
          u32x4 kk = *(const u32x4*)(kin + (size_t)(t - j) * DM + cbase);
#pragma unroll
          for (int i = 0; i < 4; ++i) {
            acc[2 * i] += bf_lo(kk[i]) * sk[2 * i][j];
            acc[2 * i + 1] += bf_hi(kk[i]) * sk[2 * i + 1][j];
          }
        }
      }
      u32x4 vv = *(const u32x4*)(vin + (size_t)t * DM + cbase);
#pragma unroll
      for (int i = 0; i < 4; ++i) {
        unsigned p = pk2(acc[2 * i] * bf_lo(vv[i]), acc[2 * i + 1] * bf_hi(vv[i]));
        Ts[(c8 * 8 + 2 * i) * 72 + tl] = (unsigned short)(p & 0xffff);
        Ts[(c8 * 8 + 2 * i + 1) * 72 + tl] = (unsigned short)(p >> 16);
      }
    }
    __syncthreads();
    {
      int cl = tid >> 2, part = tid & 3;
      const u32x4* src = (const u32x4*)(Ts + cl * 72 + part * 16);
      u32x4 a = src[0], b = src[1];
      u32x4* dst = (u32x4*)(kvT + (size_t)(ct * 64 + cl) * S_LEN + tt * 64 + part * 16);
      dst[0] = a; dst[1] = b;
    }
  } else {
    const int gb = bid - 512;
    float* G = (float*)(ws + WS_G);
    u32x4 gw = *(const u32x4*)(gatew + lane * 8);
#pragma unroll
    for (int i = 0; i < 4; ++i) {
      int t = gb * 16 + w * 4 + i;
      u32x4 xv = *(const u32x4*)(x + (size_t)t * DM + lane * 8);
      float s = 0.f;
#pragma unroll
      for (int k = 0; k < 4; ++k) s += bf_lo(xv[k]) * bf_lo(gw[k]) + bf_hi(xv[k]) * bf_hi(gw[k]);
#pragma unroll
      for (int off = 32; off >= 1; off >>= 1) s += __shfl_xor(s, off);
      if (lane == 0) G[t] = 1.f / (1.f + __expf(-s));
    }
  }
}

constexpr int K2_LDS = 78848;
constexpr float ATT_THR = 8.0f;

__global__ __launch_bounds__(512) void k2_kernel(const unsigned short* __restrict__ ssmk, char* __restrict__ ws) {
  __shared__ __attribute__((aligned(16))) char smem[K2_LDS];
  const int tid = threadIdx.x, lane = tid & 63, w = tid >> 6, r = lane & 31, h = lane >> 5;
  const int bid = blockIdx.x;
  if (bid < 256) {
    const int head = (bid & 7) * 4 + ((bid >> 3) & 3), qb = bid >> 5;
    const unsigned short* Qg = (const unsigned short*)(ws + WS_Q);
    const unsigned short* Kg = (const unsigned short*)(ws + WS_K) + (size_t)head * 64 * 512;
    const unsigned short* Vg = (const unsigned short*)(ws + WS_V) + (size_t)head * 64 * 512;
    unsigned short* Og = (unsigned short*)(ws + WS_O);
    const int q0 = qb * 256 + w * 32;
    bf16x8 qf = *(const bf16x8*)(Qg + ((size_t)head * S_LEN + q0 + r) * 16 + 8 * h);
    for (int i = tid; i < 2 * 8 * 4 * 16; i += 512) {
      int b = i >> 9, rem = i & 511, tsh = rem >> 4, row = 16 + (rem & 15);
      unsigned one = (row == 16) ? 0x3F803F80u : 0u;
      u32x4 v = {one, one, one, one};
      *(u32x4*)(smem + b * 24576 + 8192 + (tsh * 32 + row) * 16) = v;
    }
    u32x4 kr, vr;
    auto gload = [&](int ck) {
      kr = *(const u32x4*)(Kg + (size_t)ck * 4096 + tid * 8);
      vr = *(const u32x4*)(Vg + (size_t)ck * 4096 + tid * 8);
    };
    auto lwrite = [&](int b) {
      *(u32x4*)(smem + b * 24576 + tid * 16) = kr;
      int tsh = tid >> 4, d = tid & 15;
      *(u32x4*)(smem + b * 24576 + 8192 + (tsh * 32 + d) * 16) = vr;
    };
    gload(0);
    lwrite(0);
    gload(1);
    __syncthreads();
    f32x16 o, cm;
#pragma unroll
    for (int i = 0; i < 16; ++i) { o[i] = 0.f; }
    float m;
    {
      bf16x8 kf = *(const bf16x8*)(smem + (h * 32 + r) * 16);
      f32x16 z;
#pragma unroll
      for (int i = 0; i < 16; ++i) z[i] = 0.f;
      f32x16 s = MFMA32(kf, qf, z);
      float t = s[0];
#pragma unroll
      for (int i = 1; i < 16; ++i) t = fmaxf(t, s[i]);
      t = fmaxf(t, __shfl_xor(t, 32));
      m = t;
#pragma unroll
      for (int i = 0; i < 16; ++i) cm[i] = -m;
    }
    for (int ck = 0; ck < 8; ++ck) {
      const char* kb = smem + (ck & 1) * 24576;
#pragma unroll 2
      for (int t = 0; t < 8; ++t) {
        bf16x8 kf = *(const bf16x8*)(kb + t * 1024 + (h * 32 + r) * 16);
        bf16x8 vf0 = *(const bf16x8*)(kb + 8192 + ((t * 2 + 0) * 2 + h) * 512 + r * 16);
        bf16x8 vf1 = *(const bf16x8*)(kb + 8192 + ((t * 2 + 1) * 2 + h) * 512 + r * 16);
        f32x16 s = MFMA32(kf, qf, cm);
        float tm = fmaxf(fmaxf(s[0], s[1]), s[2]);
#pragma unroll
        for (int i = 3; i < 15; i += 2) tm = fmaxf(fmaxf(tm, s[i]), s[i + 1]);
        tm = fmaxf(tm, s[15]);
        if (__any(tm > ATT_THR)) {
          float tp = fmaxf(tm, __shfl_xor(tm, 32));
          float dl = fmaxf(tp, 0.f);
          float al = __builtin_amdgcn_exp2f(-dl);
          m += dl;
#pragma unroll
          for (int i = 0; i < 16; ++i) { o[i] *= al; s[i] -= dl; cm[i] = -m; }
        }
        u32x4 p0, p1;
#pragma unroll
        for (int i = 0; i < 4; ++i) {
          p0[i] = pk2(__builtin_amdgcn_exp2f(s[2 * i]), __builtin_amdgcn_exp2f(s[2 * i + 1]));
          p1[i] = pk2(__builtin_amdgcn_exp2f(s[8 + 2 * i]), __builtin_amdgcn_exp2f(s[8 + 2 * i + 1]));
        }
        o = MFMA32(vf0, __builtin_bit_cast(bf16x8, p0), o);
        o = MFMA32(vf1, __builtin_bit_cast(bf16x8, p1), o);
      }
      if (ck + 1 < 8) {
        __syncthreads();
        lwrite((ck + 1) & 1);
        if (ck + 2 < 8) gload(ck + 2);
        __syncthreads();
      }
    }
    float l = __shfl(o[8], r);
    float inv = 1.f / l;
#pragma unroll
    for (int g = 0; g < 2; ++g) {
      u32x2 v = {pk2(o[4 * g] * inv, o[4 * g + 1] * inv), pk2(o[4 * g + 2] * inv, o[4 * g + 3] * inv)};
      *(u32x2*)(Og + (size_t)(q0 + r) * DM + head * 16 + 8 * g + 4 * h) = v;
    }
  } else {
    const int cb = bid - 256, cg = w >> 2, wi = w & 3, tg = tid & 255;
    const int c = 2 * cb + cg;
    char* base = smem + cg * 39424;
    unsigned short* Ac = (unsigned short*)base;
    char* Us = base + 33280;
    const unsigned short* hg = ssmk + (size_t)c * S_LEN;
    const unsigned short* ug = (const unsigned short*)(ws + WS_KV) + (size_t)c * S_LEN;
    for (int mm = tg; mm < 260; mm += 256) {
      int i0 = 2032 - 8 * mm, i1 = 2040 - 8 * mm;
      u32x4 a = {0, 0, 0, 0}, b = {0, 0, 0, 0};
      if (i0 >= 0) a = *(const u32x4*)(hg + i0);
      if (i1 >= 0) b = *(const u32x4*)(hg + i1);
      unsigned e[9];
#pragma unroll
      for (int i = 0; i < 4; ++i) {
        e[i] = __builtin_amdgcn_alignbit(b[3 - i], b[3 - i], 16);
        e[4 + i] = __builtin_amdgcn_alignbit(a[3 - i], a[3 - i], 16);
      }
      e[8] = 0;
#pragma unroll
      for (int k = 0; k < 8; ++k) {
        u32x4 v;
        if (k & 1) {
#pragma unroll
          for (int i = 0; i < 4; ++i) v[i] = __builtin_amdgcn_alignbit(e[(k >> 1) + i + 1], e[(k >> 1) + i], 16);
        } else {
#pragma unroll
          for (int i = 0; i < 4; ++i) v[i] = e[(k >> 1) + i];
        }
        *(u32x4*)(Ac + k * 2080 + 8 * mm) = v;
      }
    }
    {
      u32x4 uv = *(const u32x4*)(ug + tg * 8);
      int row = (tg >> 2) + 32, ch = tg & 3;
      *(u32x4*)(Us + row * 64 + ((ch ^ ((row >> 2) & 3)) * 16)) = uv;
      if (tg < 128) { u32x4 z = {0, 0, 0, 0}; *(u32x4*)(Us + tg * 16) = z; }
    }
    __syncthreads();
    f32x16 a0, a1;
#pragma unroll
    for (int i = 0; i < 16; ++i) { a0[i] = 0.f; a1[i] = 0.f; }
    const unsigned short* ap = Ac + (7 - (r & 7)) * 2080 + 2040 - 8 * (r >> 3) + 8 * h;
    for (int e = wi; e < 64; e += 4) {
#pragma unroll
      for (int s = 0; s < 2; ++s) {
        bf16x8 af = *(const bf16x8*)(ap - 32 * e + 16 * s);
        int ch = 2 * s + h;
        int row1 = 64 + r - e;
        bf16x8 b1 = *(const bf16x8*)(Us + row1 * 64 + ((ch ^ ((row1 >> 2) & 3)) * 16));
        if (e < 32) {
          int row0 = 32 + r - e;
          bf16x8 b0 = *(const bf16x8*)(Us + row0 * 64 + ((ch ^ ((row0 >> 2) & 3)) * 16));
          a0 = MFMA32(af, b0, a0);
        }
        a1 = MFMA32(af, b1, a1);
      }
    }
    __syncthreads();
    float* red = (float*)base;
#pragma unroll
    for (int g = 0; g < 4; ++g) {
      f32x4 v0 = {a0[4 * g], a0[4 * g + 1], a0[4 * g + 2], a0[4 * g + 3]};
      f32x4 v1 = {a1[4 * g], a1[4 * g + 1], a1[4 * g + 2], a1[4 * g + 3]};
      *(f32x4*)(red + wi * 2048 + r * 32 + 8 * g + 4 * h) = v0;
      *(f32x4*)(red + wi * 2048 + (32 + r) * 32 + 8 * g + 4 * h) = v1;
    }
    __syncthreads();
    {
      f32x4 s0 = {0, 0, 0, 0}, s1 = {0, 0, 0, 0};
#pragma unroll
      for (int k = 0; k < 4; ++k) {
        s0 += *(const f32x4*)(red + k * 2048 + tg * 8);
        s1 += *(const f32x4*)(red + k * 2048 + tg * 8 + 4);
      }
      u32x4 v = {pk2(s0[0], s0[1]), pk2(s0[2], s0[3]), pk2(s1[0], s1[1]), pk2(s1[2], s1[3])};
      *(u32x4*)((unsigned short*)(ws + WS_Y) + (size_t)c * S_LEN + tg * 8) = v;
    }
  }
}

__global__ __launch_bounds__(256) void k3_kernel(const unsigned short* __restrict__ x,
                                                 const unsigned short* __restrict__ wo,
                                                 const unsigned short* __restrict__ ssmD,
                                                 const char* __restrict__ ws, unsigned short* __restrict__ out) {
  __shared__ __attribute__((aligned(16))) char smem[16384 + 16384];
  const int tid = threadIdx.x, lane = tid & 63, w = tid >> 6, r = lane & 31, h = lane >> 5;
  const int bid = blockIdx.x;
  const int mt = bid & 31, nt = bid >> 5;
  const int t0 = mt * 64, n0 = nt * 64;
  const unsigned short* Og = (const unsigned short*)(ws + WS_O);
  char* Xs = smem;
  char* Wsm = smem + 16384;
  u32x4 xr[2][4], wr[2][4];
  auto gload = [&](int kt, u32x4* xa, u32x4* wa) {
#pragma unroll
    for (int i = 0; i < 4; ++i) {
      int c = tid + 256 * i;
      xa[i] = *(const u32x4*)(Og + (size_t)(t0 + (c >> 4)) * DM + kt * 128 + (c & 15) * 8);
      wa[i] = *(const u32x4*)(wo + (size_t)(kt * 128 + (c >> 3)) * DM + n0 + (c & 7) * 8);
    }
  };
  auto lwrite = [&](const u32x4* xa, const u32x4* wa) {
#pragma unroll
    for (int i = 0; i < 4; ++i) {
      int c = tid + 256 * i, row = c >> 4, ch = c & 15;
      *(u32x4*)(Xs + row * 256 + ((ch ^ (row & 15)) * 16)) = xa[i];
      *(u32x4*)(Wsm + (c >> 3) * 128 + (c & 7) * 16) = wa[i];
    }
  };
  f32x16 acc;
#pragma unroll
  for (int i = 0; i < 16; ++i) acc[i] = 0.f;
  gload(0, xr[0], wr[0]);
  gload(1, xr[1], wr[1]);
  const int wm = w & 1, wn = w >> 1;
  const int xrow = 32 * wm + r;
  const int i16 = lane & 15, q4 = i16 >> 2, p4 = i16 & 3, blk = (lane >> 4) & 1;
#pragma unroll
  for (int kt = 0; kt < 4; ++kt) {
    __syncthreads();
    lwrite(xr[kt & 1], wr[kt & 1]);
    __syncthreads();
    if (kt + 2 < 4) gload(kt + 2, xr[kt & 1], wr[kt & 1]);
#pragma unroll
    for (int ks = 0; ks < 8; ++ks) {
      bf16x8 xf = *(const bf16x8*)(Xs + xrow * 256 + (((2 * ks + h) ^ (xrow & 15)) * 16));
      const char* wp = Wsm + (16 * ks + 8 * h + q4) * 128 + (32 * wn + 16 * blk + 4 * p4) * 2;
      s16x4 lo = tr_read(wp);
      s16x4 hi = tr_read(wp + 4 * 128);
      bf16x8 wf = __builtin_shufflevector(lo, hi, 0, 1, 2, 3, 4, 5, 6, 7);
      acc = MFMA32(wf, xf, acc);
    }
  }
  const int t = t0 + 32 * wm + r;
  const float g = ((const float*)(ws + WS_G))[t];
  const unsigned short* yT = (const unsigned short*)(ws + WS_Y);
  const unsigned short* kvT = (const unsigned short*)(ws + WS_KV);
#pragma unroll
  for (int gq = 0; gq < 4; ++gq) {
    int c = n0 + 32 * wn + 8 * gq + 4 * h;
    u32x2 xv = *(const u32x2*)(x + (size_t)t * DM + c);
    u32x2 dv = *(const u32x2*)(ssmD + c);
    float res[4];
#pragma unroll
    for (int i = 0; i < 4; ++i) {
      float xx = (i & 1) ? bf_hi(xv[i >> 1]) : bf_lo(xv[i >> 1]);
      float dd = (i & 1) ? bf_hi(dv[i >> 1]) : bf_lo(dv[i >> 1]);
      float y = bfs(yT[(size_t)(c + i) * S_LEN + t]);
      float kv = bfs(kvT[(size_t)(c + i) * S_LEN + t]);
      float a = xx * (y + kv * dd);
      res[i] = g * a + (1.f - g) * acc[4 * gq + i];
    }
    u32x2 v = {pk2(res[0], res[1]), pk2(res[2], res[3])};
    *(u32x2*)(out + (size_t)t * DM + c) = v;
  }
}

extern "C" void kernel_launch(void* const* d_in, const int* in_sizes, int n_in, void* d_out, int out_size,
                              void* d_ws, size_t ws_size, hipStream_t stream) {
  const unsigned short* x = (const unsigned short*)d_in[0];
  const unsigned short* kin = (const unsigned short*)d_in[1];
  const unsigned short* vin = (const unsigned short*)d_in[2];
  const unsigned short* shiftk = (const unsigned short*)d_in[3];
  const unsigned short* ssmk = (const unsigned short*)d_in[4];
  const unsigned short* ssmD = (const unsigned short*)d_in[5];
  const unsigned short* wqkv = (const unsigned short*)d_in[7];
  const unsigned short* wo = (const unsigned short*)d_in[8];
  const unsigned short* gatew = (const unsigned short*)d_in[9];
  char* ws = (char*)d_ws;
  k1_kernel<<<640, 256, 0, stream>>>(x, kin, vin, shiftk, wqkv, gatew, ws);
  k2_kernel<<<512, 512, 0, stream>>>(ssmk, ws);
  k3_kernel<<<256, 256, 0, stream>>>(x, wo, ssmD, ws, (unsigned short*)d_out);
}
```
